```python
import torch

def _softmax(x, axis=-1):
    return torch.softmax(x, dim=axis)

def _gelu(x):
    return torch.nn.functional.gelu(x, approximate='tanh')

def _silu(x):
    return torch.nn.functional.silu(x)

def _sigmoid(x):
    return torch.sigmoid(x)

def _layernorm(x, axis=-1, eps=1e-05):
    m = x.mean(dim=axis, keepdim=True)
    v = x.var(dim=axis, keepdim=True, unbiased=False)
    return (x - m) / torch.sqrt(v + eps)

def _scatter_add(tgt, idx, src, axis=0):
    return tgt.clone().index_add_(axis, idx, src)

def _segment_sum(src, seg, n):
    out = torch.zeros((n,) + src.shape[1:], dtype=src.dtype)
    return out.index_add_(0, seg, src)

def _pad(x, pw):
    flat = tuple((v for pair in reversed(pw) for v in pair))
    return torch.nn.functional.pad(x, flat)
kernel_name = 'standalone__autoregressive_conv_wavenet__speech'

def setup_inputs():

    _w = lambda a: (lambda t: t.to(torch.bfloat16) if t.is_floating_point() else t)(torch.as_tensor(a))
    B, T, D, K = (2, 1000, 512, 3)
    return {'x': _w((lambda *s: torch.randn(*s, dtype=torch.bfloat16) * 0.1)(B, T, D)), 'w': _w((lambda *s: torch.randn(*s, dtype=torch.bfloat16) * 0.1)(K, D, 2 * D)), 'w_o': _w((lambda *s: torch.randn(*s, dtype=torch.bfloat16) * 0.05)(D, D))}

def reference(x, w, w_o):
    B, T, D = x.shape
    K = w.shape[0]
    dil = 2
    pad = (K - 1) * dil
    xp = _pad(x, ((0, 0), (pad, 0), (0, 0)))
    h = torch.einsum('btc,cd->btd', xp[:, pad:pad + T, :], w[0])
    for i in range(1, K):
        off = pad - i * dil
        h = h + torch.einsum('btc,cd->btd', xp[:, off:off + T, :], w[i])
    f = h[..., :D].clone()
    g = h[..., D:].clone()
    z = torch.tanh(f) * _sigmoid(g)
    return z @ w_o
```

```cpp
#include <hip/hip_runtime.h>
#include <hip/hip_bf16.h>
typedef unsigned short u16;
__device__ __forceinline__ float bf2f(u16 v){ return __uint_as_float(((unsigned)v)<<16); }
__device__ __forceinline__ u16 f2bf(float f){ __hip_bfloat16 b = __float2bfloat16(f); return *(u16*)&b; }
constexpr int B=2,T=1000,D=512;
__global__ void k1(const u16* x,const u16* w,u16* z){
  int idx=blockIdx.x*blockDim.x+threadIdx.x;
  int d=idx%D; int bt=idx/D; int t=bt%T;
  float f=0,g=0;
  for(int i=0;i<3;i++){ int tt=t-2*i; if(tt<0) continue;
    const u16* xr=x+(size_t)(bt-2*i)*D; const u16* wr=w+(size_t)i*D*2*D;
    for(int c=0;c<D;c++){ float xv=bf2f(xr[c]); f+=xv*bf2f(wr[(size_t)c*2*D+d]); g+=xv*bf2f(wr[(size_t)c*2*D+D+d]); }
  }
  float zz=tanhf(f)*(1.f/(1.f+__expf(-g)));
  z[idx]=f2bf(zz);
}
__global__ void k2(const u16* z,const u16* wo,u16* out){
  int idx=blockIdx.x*blockDim.x+threadIdx.x; int d=idx%D; int bt=idx/D;
  float a=0; for(int c=0;c<D;c++) a+=bf2f(z[(size_t)bt*D+c])*bf2f(wo[(size_t)c*D+d]);
  out[idx]=f2bf(a);
}
extern "C" void kernel_launch(void* const* d_in, const int* in_sizes, int n_in, void* d_out, int out_size, void* d_ws, size_t ws_size, hipStream_t stream) {
  const u16* x=(const u16*)d_in[0]; const u16* w=(const u16*)d_in[1]; const u16* wo=(const u16*)d_in[2];
  u16* z=(u16*)d_ws; u16* out=(u16*)d_out;
  k1<<<B*T*D/256,256,0,stream>>>(x,w,z);
  k2<<<B*T*D/256,256,0,stream>>>(z,wo,out);
}
```

```cpp
#include <hip/hip_runtime.h>
#include <stdint.h>

typedef unsigned int u32;
typedef unsigned long long u64;
typedef __attribute__((address_space(3))) char* lds_t;
typedef float f32x4 __attribute__((ext_vector_type(4)));
typedef float f32x2 __attribute__((ext_vector_type(2)));
typedef __bf16 bf16x8 __attribute__((ext_vector_type(8)));
typedef __bf16 bf16x2 __attribute__((ext_vector_type(2)));
typedef short s16x4 __attribute__((ext_vector_type(4)));
typedef u32 u32x4 __attribute__((ext_vector_type(4)));
typedef u32 u32x2 __attribute__((ext_vector_type(2)));

#define FUSED 1

constexpr int XS = 17 * 1024, WS = 24 * 1024, SLOT = XS + WS;
constexpr u32 EXB = 16u * 128u * 512u * 2u;
constexpr u32 FLAG_OFF = 2u * EXB;
constexpr u32 EPOCH_OFF = FLAG_OFF + 4096;
constexpr u32 STAMP_OFF = FLAG_OFF + 65536;
constexpr int RA = 131072, RB = 98304, RC = 0, RD = 32768, RE = 65536;

#define LDSP(T, a) ((__attribute__((address_space(3))) T*)(uintptr_t)(a))
#define BAR() asm volatile("s_waitcnt lgkmcnt(0)\n\ts_barrier" ::: "memory")
template <int N> __device__ __forceinline__ void wait_vm() { asm volatile("s_waitcnt vmcnt(%0)" ::"n"(N) : "memory"); }

#define STAMP(i)
#define STAMPC(i)

struct Frags { bf16x8 x[6]; bf16x8 w[3][2]; };

__device__ __forceinline__ bf16x8 tr_frag(u32 a) {
  s16x4 lo = __builtin_amdgcn_ds_read_tr16_b64_v4i16(LDSP(s16x4, a));
  s16x4 hi = __builtin_amdgcn_ds_read_tr16_b64_v4i16(LDSP(s16x4, a + 512));
  typedef short s16x8 __attribute__((ext_vector_type(8)));
  s16x8 v = __builtin_shufflevector(lo, hi, 0, 1, 2, 3, 4, 5, 6, 7);
  return __builtin_bit_cast(bf16x8, v);
}
__device__ __forceinline__ bf16x8 tr_frag2(u32 a) {
  s16x4 lo = __builtin_amdgcn_ds_read_tr16_b64_v4i16(LDSP(s16x4, a));
  s16x4 hi = __builtin_amdgcn_ds_read_tr16_b64_v4i16(LDSP(s16x4, a + 1024));
  typedef short s16x8 __attribute__((ext_vector_type(8)));
  s16x8 v = __builtin_shufflevector(lo, hi, 0, 1, 2, 3, 4, 5, 6, 7);
  return __builtin_bit_cast(bf16x8, v);
}

__device__ __forceinline__ void load_frags(Frags& F, const u32 (&ax)[2][2], const u32 (&aw)[2], int ks, u32 sb) {
#pragma unroll
  for (int jj = 0; jj < 6; jj++) {
    const int win = jj >> 2, se = (2 * jj) & 7;
    F.x[jj] = *LDSP(bf16x8, ax[win][ks] + sb + se * 17 * 128);
  }
#pragma unroll
  for (int i = 0; i < 3; i++)
#pragma unroll
    for (int T = 0; T < 2; T++) F.w[i][T] = tr_frag(aw[T] + sb + XS + i * 8192 + ks * 4096);
}
__device__ __forceinline__ void mfma_block(f32x4 (&acc)[4][2], const Frags& F) {
#pragma unroll
  for (int i = 0; i < 3; i++)
#pragma unroll
    for (int j = 0; j < 4; j++)
#pragma unroll
      for (int T = 0; T < 2; T++)
        acc[j][T] = __builtin_amdgcn_mfma_f32_16x16x32_bf16(F.w[i][T], F.x[j - i + 2], acc[j][T], 0, 0, 0);
}
__device__ __forceinline__ void sched_mix() {
#pragma unroll
  for (int i = 0; i < 18; i++) {
    __builtin_amdgcn_sched_group_barrier(0x008, 1, 0);
    __builtin_amdgcn_sched_group_barrier(0x100, 1, 0);
  }
  __builtin_amdgcn_sched_group_barrier(0x008, 6, 0);
}
__device__ __forceinline__ u32 pk_bf16(float a, float b) {
  f32x2 p = {a, b};
  return __builtin_bit_cast(u32, __builtin_convertvector(p, bf16x2));
}
__device__ __forceinline__ u32 mul_bf16x2(u32 a, u32 b) {
  float al = __uint_as_float(a << 16), ah = __uint_as_float(a & 0xffff0000u);
  float bl = __uint_as_float(b << 16), bh = __uint_as_float(b & 0xffff0000u);
  return pk_bf16(al * bl, ah * bh);
}

template <int MODE>
__global__ __launch_bounds__(512, 2) void wn_kernel(const char* __restrict__ x, const char* __restrict__ w,
                                                    const char* __restrict__ wo, char* __restrict__ out,
                                                    char* __restrict__ ws) {
  __shared__ __attribute__((aligned(1024))) char lds_raw[163840];
  const u32 lb = (u32)(uintptr_t)(lds_t)lds_raw;
  const int tid = threadIdx.x, l = tid & 63;
  const int wave = __builtin_amdgcn_readfirstlane(tid >> 6);
  const int bid = blockIdx.x, xcd = bid & 7, idx = bid >> 3;
  const int rt = 8 * (xcd >> 2) + (idx >> 2), ct = 4 * (xcd & 3) + (idx & 3);
  const int g = l >> 4, r = l & 15;
  constexpr int AUXS = (MODE == 2) ? 16 : 0;
  STAMP(0); STAMPC(1);

  auto rex = __builtin_amdgcn_make_buffer_rsrc((void*)ws, (short)0, (int)(2u * EXB), 0x00020000);
  u64 epoch = 0;
  u64* flags = (u64*)(ws + FLAG_OFF + rt * 128);
  u64* epw = (u64*)(ws + EPOCH_OFF + rt * 128);

  if (wave >= 4) {
    const int L = wave - 4;
    auto rwo = __builtin_amdgcn_make_buffer_rsrc((void*)wo, (short)0, 512 * 1024, 0x00020000);
    const int cc = ct & 3;
    u32 voo[2];
#pragma unroll
    for (int e = 0; e < 2; e++) voo[e] = (u32)((l >> 4) * 1024 + cc * 256 + (((l & 15) ^ (2 * ((l >> 4) + 4 * e))) * 16));
    auto issue_wo = [&](int piece, int region) {
#pragma unroll
      for (int i = 0; i < 8; i++)
        __builtin_amdgcn_raw_ptr_buffer_load_lds(rwo, LDSP(void, lb + region + (L + 4 * i) * 1024), 16, voo[i & 1],
                                                 (piece * 128 + 4 * (L + 4 * i)) * 1024, 0, 0);
    };
    if (MODE != 1) {
      auto rx = __builtin_amdgcn_make_buffer_rsrc((void*)x, (short)0, 2000 * 1024, 0x00020000);
      auto rw = __builtin_amdgcn_make_buffer_rsrc((void*)w, (short)0, 1536 * 2048, 0x00020000);
      u32 vox[5];
#pragma unroll
      for (int i = 0; i < 5; i++) {
        const int pi = 8 * (L + 4 * i) + (l >> 3);
        const int s = pi / 17, rho = pi - 17 * s, u = 8 * rho + s;
        const int row = 125 * rt - 4 + u;
        const bool ok = (u >= 4 || (rt & 7) != 0) && row < 2000;
        vox[i] = ok ? (u32)(row * 1024 + (((l & 7) ^ (((rho >> 1) & 3) << 1)) * 16)) : 0x80000000u;
      }
      const int h = ((l >> 4) & 1) | ((L & 1) << 1);
      const u32 vow = (u32)((l >> 3) * 2048 + ct * 128 + (((l & 7) ^ (h << 1)) * 16));
      auto issue = [&](int s) {
        const u32 sb = lb + (s % 3) * SLOT;
#pragma unroll
        for (int i = 0; i < 4; i++)
          __builtin_amdgcn_raw_ptr_buffer_load_lds(rx, LDSP(void, sb + (L + 4 * i) * 1024), 16, vox[i], s * 128, 0, 0);
        if (L == 0) __builtin_amdgcn_raw_ptr_buffer_load_lds(rx, LDSP(void, sb + 16 * 1024), 16, vox[4], s * 128, 0, 0);
#pragma unroll
        for (int q = 0; q < 6; q++)
          __builtin_amdgcn_raw_ptr_buffer_load_lds(rw, LDSP(void, sb + XS + (L + 4 * q) * 1024), 16, vow,
                                                   ((q >> 1) * 512 + 8 * L + 32 * (q & 1) + s * 64) * 2048, 0, 0);
      };
#define WAITN(c, e) do { if (L == 0) wait_vm<11 * (c) + (e)>(); else wait_vm<10 * (c) + (e)>(); } while (0)
      issue(0); issue(1); issue(2);
      WAITN(2, 0); STAMPC(2); BAR();
      WAITN(1, 0); STAMPC(3); BAR(); issue(3);
      WAITN(1, 0); STAMPC(4); BAR(); issue(4);
      WAITN(1, 0); STAMPC(5); BAR(); issue(5);
      WAITN(1, 0); STAMPC(6); BAR(); issue(6);
      WAITN(1, 0); STAMPC(7); BAR(); issue(7);
      if (MODE == 2) issue_wo(0, RA);
      if (MODE == 2) WAITN(1, 8); else WAITN(1, 0);
      STAMPC(8); BAR();
      if (MODE == 2) { issue_wo(1, RB); wait_vm<16>(); } else wait_vm<0>();
      STAMPC(9); BAR();
      if (MODE == 2) issue_wo(2, RC);
      BAR();
      if (MODE == 2) issue_wo(3, RD);
    } else {
      issue_wo(0, RA); issue_wo(1, RB); issue_wo(2, RC); issue_wo(3, RD);
    }
    if (MODE != 0) {
      wait_vm<0>(); STAMPC(10);
      BAR();
      bf16x8 wf[16][2];
      {
        const int q = (l & 15) >> 2, p = l & 3, pg = ((g & 1) << 1) | (g >> 1);
        u32 a[2];
#pragma unroll
        for (int T = 0; T < 2; T++)
          a[T] = lb + (8 * pg + q) * 256 + (((4 * L + 2 * T + (p >> 1)) ^ (2 * (q + 4 * (g & 1)))) * 16) + 8 * (p & 1);
#pragma unroll
        for (int k = 0; k < 16; k++) {
          const int reg = (k >> 2) == 0 ? RA : (k >> 2) == 1 ? RB : (k >> 2) == 2 ? RC : RD;
#pragma unroll
          for (int T = 0; T < 2; T++) wf[k][T] = tr_frag2(a[T] + reg + (k & 3) * 32 * 256);
        }
      }
      STAMPC(11);
      BAR();
      f32x4 acc[2][2] = {};
#pragma unroll
      for (int q4 = 0; q4 < 4; q4++) {
        BAR();
        bf16x8 zf[4][2];
#pragma unroll
        for (int k = 0; k < 4; k++)
#pragma unroll
          for (int jl = 0; jl < 2; jl++) zf[k][jl] = *LDSP(bf16x8, lb + RE + ((q4 * 4 + k) * 2 + jl) * 1024 + l * 16);
#pragma unroll
        for (int k = 0; k < 4; k++)
#pragma unroll
          for (int jl = 0; jl < 2; jl++)
#pragma unroll
            for (int T = 0; T < 2; T++)
              acc[jl][T] = __builtin_amdgcn_mfma_f32_16x16x32_bf16(wf[q4 * 4 + k][T], zf[k][jl], acc[jl][T], 0, 0, 0);
      }
      STAMPC(12);
      auto ro = __builtin_amdgcn_make_buffer_rsrc((void*)out, (short)0, 2000 * 1024, 0x00020000);
      const int rc = ct >> 2, p = rc & 1, jh = rc >> 1;
      const int cm = ((g & 1) << 4) | ((g >> 1) << 3);
#pragma unroll
      for (int jl = 0; jl < 2; jl++) {
        const int tl = 8 * r + 2 * (2 * jh + jl) + p;
        u32 x0 = pk_bf16(acc[jl][0][0], acc[jl][0][1]), x1 = pk_bf16(acc[jl][0][2], acc[jl][0][3]);
        u32 y0 = pk_bf16(acc[jl][1][0], acc[jl][1][1]), y1 = pk_bf16(acc[jl][1][2], acc[jl][1][3]);
        auto s0 = __builtin_amdgcn_permlane16_swap(x0, y0, false, false);
        auto s1 = __builtin_amdgcn_permlane16_swap(x1, y1, false, false);
        u32x4 v = {s0[0], s1[0], s0[1], s1[1]};
        const u32 off = tl < 125 ? (u32)((125 * rt + tl) * 1024 + (cc * 128 + 32 * L + cm) * 2) : 0x80000000u;
        __builtin_amdgcn_raw_buffer_store_b128(v, ro, off, 0, 0);
      }
      STAMPC(13);
    }
  } else {
    const int p = wave & 1, ch = wave >> 1;
    if (MODE == 2) epoch = __hip_atomic_load(epw, __ATOMIC_RELAXED, __HIP_MEMORY_SCOPE_AGENT);
    if (MODE != 1) {
      u32 ax[2][2], aw[2];
#pragma unroll
      for (int win = 0; win < 2; win++)
#pragma unroll
        for (int ks = 0; ks < 2; ks++) {
          const int rho = r + win;
          ax[win][ks] = lb + (p * 17 + rho) * 128 + (((4 * ks + g) ^ (((rho >> 1) & 3) << 1)) * 16);
        }
      {
        const int q = (l & 15) >> 2, pp = l & 3, h = ((q >> 1) & 1) | ((g & 1) << 1);
#pragma unroll
        for (int T = 0; T < 2; T++)
          aw[T] = lb + (8 * g + q) * 128 + (((2 * (2 * ch + T) + (pp >> 1)) ^ (h << 1)) * 16) + 8 * (pp & 1);
      }
      f32x4 acc[4][2] = {};
      Frags F0, F1;
      BAR();
      STAMPC(2);
      load_frags(F0, ax, aw, 0, 0);
#pragma unroll
      for (int s = 0; s < 8; s++) {
        __builtin_amdgcn_sched_barrier(0);
        load_frags(F1, ax, aw, 1, (s % 3) * SLOT);
        mfma_block(acc, F0);
        sched_mix();
        __builtin_amdgcn_sched_barrier(0);
        BAR();
        if (s == 0 || s == 3 || s == 6) STAMPC(3 + s / 3);
        __builtin_amdgcn_sched_barrier(0);
        if (s < 7) load_frags(F0, ax, aw, 0, ((s + 1) % 3) * SLOT);
        mfma_block(acc, F1);
        if (s < 7) sched_mix();
      }
      __builtin_amdgcn_sched_barrier(0);
      STAMPC(6);
      const bool isF = ct < 8;
      const float c1 = isF ? 2.885390081777927f : -1.4426950408889634f;
      const float c2 = isF ? -2.f : 1.f, c3 = isF ? 1.f : 0.f;
      const u32 exb = (isF ? 0u : EXB) + (u32)((ct & 7) * 4 + ch * 2) * 1024u + (u32)(rt * 4) * 32768u;
#pragma unroll
      for (int j = 0; j < 4; j++) {
        u32 d[2][2];
#pragma unroll
        for (int T = 0; T < 2; T++) {
          float v[4];
#pragma unroll
          for (int e = 0; e < 4; e++) {
            const float t = __builtin_amdgcn_exp2f(acc[j][T][e] * c1);
            v[e] = __builtin_fmaf(__builtin_amdgcn_rcpf(t + 1.f), c2, c3);
          }
          d[T][0] = pk_bf16(v[0], v[1]); d[T][1] = pk_bf16(v[2], v[3]);
        }
        auto s0 = __builtin_amdgcn_permlane16_swap(d[0][0], d[1][0], false, false);
        auto s1 = __builtin_amdgcn_permlane16_swap(d[0][1], d[1][1], false, false);
        u32x4 v = {s0[0], s1[0], s0[1], s1[1]};
        const int rc = (j >> 1) * 2 + p;
        __builtin_amdgcn_raw_buffer_store_b128(v, rex, l * 16, exb + rc * 32768 + (j & 1) * 1024, AUXS);
      }
      STAMPC(7);
    }
    if (MODE != 0) {
      if (MODE == 2) wait_vm<0>();
      STAMPC(8);
      BAR();
      if (MODE == 2 && wave == 0) {
        const u64 tag = epoch + 1;
        if (l == 0) __hip_atomic_store(flags + ct, tag, __ATOMIC_RELAXED, __HIP_MEMORY_SCOPE_AGENT);
        int spins = 0;
        while (true) {
          u64 v = tag;
          if (l < 16) v = __hip_atomic_load(flags + l, __ATOMIC_RELAXED, __HIP_MEMORY_SCOPE_AGENT);
          if (__builtin_amdgcn_ballot_w64(v != tag) == 0) break;
          if (++spins > (1 << 20)) break;
          __builtin_amdgcn_s_sleep(1);
        }
        if (ct == 0 && l == 0) __hip_atomic_store(epw, tag, __ATOMIC_RELAXED, __HIP_MEMORY_SCOPE_AGENT);
      }
      STAMPC(9);
      BAR();
      const int rc = ct >> 2;
      const u32 base = (u32)(rt * 4 + rc) * 32768u;
      u32x4 av[4][2], bv[4][2];
#pragma unroll
      for (int q4 = 0; q4 < 4; q4++)
#pragma unroll
        for (int jl = 0; jl < 2; jl++) {
          const u32 o = base + (u32)(((q4 * 4 + wave) * 2 + jl) * 1024);
          av[q4][jl] = __builtin_amdgcn_raw_buffer_load_b128(rex, l * 16, o, AUXS);
          bv[q4][jl] = __builtin_amdgcn_raw_buffer_load_b128(rex, l * 16, o + EXB, AUXS);
        }
#pragma unroll
      for (int q4 = 0; q4 < 4; q4++) {
#pragma unroll
        for (int jl = 0; jl < 2; jl++) {
          u32x4 z;
#pragma unroll
          for (int e = 0; e < 4; e++) z[e] = mul_bf16x2(av[q4][jl][e], bv[q4][jl][e]);
          *LDSP(u32x4, lb + RE + ((q4 * 4 + wave) * 2 + jl) * 1024 + l * 16) = z;
        }
        BAR();
      }
      STAMPC(10);
    }
  }
}

extern "C" void kernel_launch(void* const* d_in, const int* in_sizes, int n_in, void* d_out, int out_size, void* d_ws,
                              size_t ws_size, hipStream_t stream) {
  const char* x = (const char*)d_in[0];
  const char* w = (const char*)d_in[1];
  const char* wo = (const char*)d_in[2];
  wn_kernel<2><<<256, 512, 0, stream>>>(x, w, wo, (char*)d_out, (char*)d_ws);
}
```

```cpp
#define PADK 99
#define PADW 0
#include <hip/hip_runtime.h>
#include <stdint.h>

typedef unsigned int u32;
typedef unsigned long long u64;
typedef __attribute__((address_space(3))) char* lds_t;
typedef float f32x4 __attribute__((ext_vector_type(4)));
typedef float f32x2 __attribute__((ext_vector_type(2)));
typedef __bf16 bf16x8 __attribute__((ext_vector_type(8)));
typedef __bf16 bf16x2 __attribute__((ext_vector_type(2)));
typedef short s16x4 __attribute__((ext_vector_type(4)));
typedef u32 u32x4 __attribute__((ext_vector_type(4)));
typedef u32 u32x2 __attribute__((ext_vector_type(2)));

#define FUSED 1

constexpr int XS = 17 * 1024, WS = 24 * 1024, SLOT = XS + WS;
constexpr u32 EXB = 16u * 128u * 512u * 2u;
constexpr u32 FLAG_OFF = 2u * EXB;
constexpr u32 EPOCH_OFF = FLAG_OFF + 4096;
constexpr u32 STAMP_OFF = FLAG_OFF + 65536;
constexpr int RA = 131072, RB = 98304, RC = 0, RD = 32768, RE = 65536;

#define LDSP(T, a) ((__attribute__((address_space(3))) T*)(uintptr_t)(a))
#define BAR() asm volatile("s_waitcnt lgkmcnt(0)\n\ts_barrier" ::: "memory")
template <int N> __device__ __forceinline__ void wait_vm() { asm volatile("s_waitcnt vmcnt(%0)" ::"n"(N) : "memory"); }

#define STAMP(i) do { if ((i) == 0) tpad0 = __builtin_amdgcn_s_memrealtime(); } while (0)
#define STAMPC(i) do { if ((i) == PADK) tpadk = __builtin_amdgcn_s_memrealtime(); } while (0)

struct Frags { bf16x8 x[6]; bf16x8 w[3][2]; };

__device__ __forceinline__ bf16x8 tr_frag(u32 a) {
  s16x4 lo = __builtin_amdgcn_ds_read_tr16_b64_v4i16(LDSP(s16x4, a));
  s16x4 hi = __builtin_amdgcn_ds_read_tr16_b64_v4i16(LDSP(s16x4, a + 512));
  typedef short s16x8 __attribute__((ext_vector_type(8)));
  s16x8 v = __builtin_shufflevector(lo, hi, 0, 1, 2, 3, 4, 5, 6, 7);
  return __builtin_bit_cast(bf16x8, v);
}
__device__ __forceinline__ bf16x8 tr_frag2(u32 a) {
  s16x4 lo = __builtin_amdgcn_ds_read_tr16_b64_v4i16(LDSP(s16x4, a));
  s16x4 hi = __builtin_amdgcn_ds_read_tr16_b64_v4i16(LDSP(s16x4, a + 1024));
  typedef short s16x8 __attribute__((ext_vector_type(8)));
  s16x8 v = __builtin_shufflevector(lo, hi, 0, 1, 2, 3, 4, 5, 6, 7);
  return __builtin_bit_cast(bf16x8, v);
}

__device__ __forceinline__ void load_frags(Frags& F, const u32 (&ax)[2][2], const u32 (&aw)[2], int ks, u32 sb) {
#pragma unroll
  for (int jj = 0; jj < 6; jj++) {
    const int win = jj >> 2, se = (2 * jj) & 7;
    F.x[jj] = *LDSP(bf16x8, ax[win][ks] + sb + se * 17 * 128);
  }
#pragma unroll
  for (int i = 0; i < 3; i++)
#pragma unroll
    for (int T = 0; T < 2; T++) F.w[i][T] = tr_frag(aw[T] + sb + XS + i * 8192 + ks * 4096);
}
__device__ __forceinline__ void mfma_block(f32x4 (&acc)[4][2], const Frags& F) {
#pragma unroll
  for (int i = 0; i < 3; i++)
#pragma unroll
    for (int j = 0; j < 4; j++)
#pragma unroll
      for (int T = 0; T < 2; T++)
        acc[j][T] = __builtin_amdgcn_mfma_f32_16x16x32_bf16(F.w[i][T], F.x[j - i + 2], acc[j][T], 0, 0, 0);
}
__device__ __forceinline__ void sched_mix() {
#pragma unroll
  for (int i = 0; i < 18; i++) {
    __builtin_amdgcn_sched_group_barrier(0x008, 1, 0);
    __builtin_amdgcn_sched_group_barrier(0x100, 1, 0);
  }
  __builtin_amdgcn_sched_group_barrier(0x008, 6, 0);
}
__device__ __forceinline__ u32 pk_bf16(float a, float b) {
  f32x2 p = {a, b};
  return __builtin_bit_cast(u32, __builtin_convertvector(p, bf16x2));
}
__device__ __forceinline__ u32 mul_bf16x2(u32 a, u32 b) {
  float al = __uint_as_float(a << 16), ah = __uint_as_float(a & 0xffff0000u);
  float bl = __uint_as_float(b << 16), bh = __uint_as_float(b & 0xffff0000u);
  return pk_bf16(al * bl, ah * bh);
}

template <int MODE>
__global__ __launch_bounds__(512, 2) void wn_kernel(const char* __restrict__ x, const char* __restrict__ w,
                                                    const char* __restrict__ wo, char* __restrict__ out,
                                                    char* __restrict__ ws) {
  __shared__ __attribute__((aligned(1024))) char lds_raw[163840];
  const u32 lb = (u32)(uintptr_t)(lds_t)lds_raw;
  const int tid = threadIdx.x, l = tid & 63;
  const int wave = __builtin_amdgcn_readfirstlane(tid >> 6);
  const int bid = blockIdx.x, xcd = bid & 7, idx = bid >> 3;
  const int rt = 8 * (xcd >> 2) + (idx >> 2), ct = 4 * (xcd & 3) + (idx & 3);
  const int g = l >> 4, r = l & 15;
  constexpr int AUXS = (MODE == 2) ? 16 : 0;
  u64 tpad0 = 0, tpadk = 0;
  STAMP(0); STAMPC(1);

  auto rex = __builtin_amdgcn_make_buffer_rsrc((void*)ws, (short)0, (int)(2u * EXB), 0x00020000);
  u64 epoch = 0;
  u64* flags = (u64*)(ws + FLAG_OFF + rt * 128);
  u64* epw = (u64*)(ws + EPOCH_OFF + rt * 128);

  if (wave >= 4) {
    const int L = wave - 4;
    auto rwo = __builtin_amdgcn_make_buffer_rsrc((void*)wo, (short)0, 512 * 1024, 0x00020000);
    const int cc = ct & 3;
    u32 voo[2];
#pragma unroll
    for (int e = 0; e < 2; e++) voo[e] = (u32)((l >> 4) * 1024 + cc * 256 + (((l & 15) ^ (2 * ((l >> 4) + 4 * e))) * 16));
    auto issue_wo = [&](int piece, int region) {
#pragma unroll
      for (int i = 0; i < 8; i++)
        __builtin_amdgcn_raw_ptr_buffer_load_lds(rwo, LDSP(void, lb + region + (L + 4 * i) * 1024), 16, voo[i & 1],
                                                 (piece * 128 + 4 * (L + 4 * i)) * 1024, 0, 0);
    };
    if (MODE != 1) {
      auto rx = __builtin_amdgcn_make_buffer_rsrc((void*)x, (short)0, 2000 * 1024, 0x00020000);
      auto rw = __builtin_amdgcn_make_buffer_rsrc((void*)w, (short)0, 1536 * 2048, 0x00020000);
      u32 vox[5];
#pragma unroll
      for (int i = 0; i < 5; i++) {
        const int pi = 8 * (L + 4 * i) + (l >> 3);
        const int s = pi / 17, rho = pi - 17 * s, u = 8 * rho + s;
        const int row = 125 * rt - 4 + u;
        const bool ok = (u >= 4 || (rt & 7) != 0) && row < 2000;
        vox[i] = ok ? (u32)(row * 1024 + (((l & 7) ^ (((rho >> 1) & 3) << 1)) * 16)) : 0x80000000u;
      }
      const int h = ((l >> 4) & 1) | ((L & 1) << 1);
      const u32 vow = (u32)((l >> 3) * 2048 + ct * 128 + (((l & 7) ^ (h << 1)) * 16));
      auto issue = [&](int s) {
        const u32 sb = lb + (s % 3) * SLOT;
#pragma unroll
        for (int i = 0; i < 4; i++)
          __builtin_amdgcn_raw_ptr_buffer_load_lds(rx, LDSP(void, sb + (L + 4 * i) * 1024), 16, vox[i], s * 128, 0, 0);
        if (L == 0) __builtin_amdgcn_raw_ptr_buffer_load_lds(rx, LDSP(void, sb + 16 * 1024), 16, vox[4], s * 128, 0, 0);
#pragma unroll
        for (int q = 0; q < 6; q++)
          __builtin_amdgcn_raw_ptr_buffer_load_lds(rw, LDSP(void, sb + XS + (L + 4 * q) * 1024), 16, vow,
                                                   ((q >> 1) * 512 + 8 * L + 32 * (q & 1) + s * 64) * 2048, 0, 0);
      };
#define WAITN(c, e) do { if (L == 0) wait_vm<11 * (c) + (e)>(); else wait_vm<10 * (c) + (e)>(); } while (0)
      issue(0); issue(1); issue(2);
      WAITN(2, 0); STAMPC(2); BAR();
      WAITN(1, 0); STAMPC(3); BAR(); issue(3);
      WAITN(1, 0); STAMPC(4); BAR(); issue(4);
      WAITN(1, 0); STAMPC(5); BAR(); issue(5);
      WAITN(1, 0); STAMPC(6); BAR(); issue(6);
      WAITN(1, 0); STAMPC(7); BAR(); issue(7);
      if (MODE == 2) issue_wo(0, RA);
      if (MODE == 2) WAITN(1, 8); else WAITN(1, 0);
      STAMPC(8); BAR();
      if (MODE == 2) { issue_wo(1, RB); wait_vm<16>(); } else wait_vm<0>();
      STAMPC(9); BAR();
      if (MODE == 2) issue_wo(2, RC);
      BAR();
      if (MODE == 2) issue_wo(3, RD);
    } else {
      issue_wo(0, RA); issue_wo(1, RB); issue_wo(2, RC); issue_wo(3, RD);
    }
    if (MODE != 0) {
      wait_vm<0>(); STAMPC(10);
      BAR();
      bf16x8 wf[16][2];
      {
        const int q = (l & 15) >> 2, p = l & 3, pg = ((g & 1) << 1) | (g >> 1);
        u32 a[2];
#pragma unroll
        for (int T = 0; T < 2; T++)
          a[T] = lb + (8 * pg + q) * 256 + (((4 * L + 2 * T + (p >> 1)) ^ (2 * (q + 4 * (g & 1)))) * 16) + 8 * (p & 1);
#pragma unroll
        for (int k = 0; k < 16; k++) {
          const int reg = (k >> 2) == 0 ? RA : (k >> 2) == 1 ? RB : (k >> 2) == 2 ? RC : RD;
#pragma unroll
          for (int T = 0; T < 2; T++) wf[k][T] = tr_frag2(a[T] + reg + (k & 3) * 32 * 256);
        }
      }
      STAMPC(11);
      BAR();
      f32x4 acc[2][2] = {};
#pragma unroll
      for (int q4 = 0; q4 < 4; q4++) {
        BAR();
        bf16x8 zf[4][2];
#pragma unroll
        for (int k = 0; k < 4; k++)
#pragma unroll
          for (int jl = 0; jl < 2; jl++) zf[k][jl] = *LDSP(bf16x8, lb + RE + ((q4 * 4 + k) * 2 + jl) * 1024 + l * 16);
#pragma unroll
        for (int k = 0; k < 4; k++)
#pragma unroll
          for (int jl = 0; jl < 2; jl++)
#pragma unroll
            for (int T = 0; T < 2; T++)
              acc[jl][T] = __builtin_amdgcn_mfma_f32_16x16x32_bf16(wf[q4 * 4 + k][T], zf[k][jl], acc[jl][T], 0, 0, 0);
      }
      STAMPC(12);
      auto ro = __builtin_amdgcn_make_buffer_rsrc((void*)out, (short)0, 2000 * 1024, 0x00020000);
      const int rc = ct >> 2, p = rc & 1, jh = rc >> 1;
      const int cm = ((g & 1) << 4) | ((g >> 1) << 3);
#pragma unroll
      for (int jl = 0; jl < 2; jl++) {
        const int tl = 8 * r + 2 * (2 * jh + jl) + p;
        u32 x0 = pk_bf16(acc[jl][0][0], acc[jl][0][1]), x1 = pk_bf16(acc[jl][0][2], acc[jl][0][3]);
        u32 y0 = pk_bf16(acc[jl][1][0], acc[jl][1][1]), y1 = pk_bf16(acc[jl][1][2], acc[jl][1][3]);
        auto s0 = __builtin_amdgcn_permlane16_swap(x0, y0, false, false);
        auto s1 = __builtin_amdgcn_permlane16_swap(x1, y1, false, false);
        u32x4 v = {s0[0], s1[0], s0[1], s1[1]};
        const u32 off = tl < 125 ? (u32)((125 * rt + tl) * 1024 + (cc * 128 + 32 * L + cm) * 2) : 0x80000000u;
        __builtin_amdgcn_raw_buffer_store_b128(v, ro, off, 0, 16);
      }
      STAMPC(13);
    }
  } else {
    const int p = wave & 1, ch = wave >> 1;
    if (MODE == 2) epoch = __hip_atomic_load(epw, __ATOMIC_RELAXED, __HIP_MEMORY_SCOPE_AGENT);
    if (MODE != 1) {
      u32 ax[2][2], aw[2];
#pragma unroll
      for (int win = 0; win < 2; win++)
#pragma unroll
        for (int ks = 0; ks < 2; ks++) {
          const int rho = r + win;
          ax[win][ks] = lb + (p * 17 + rho) * 128 + (((4 * ks + g) ^ (((rho >> 1) & 3) << 1)) * 16);
        }
      {
        const int q = (l & 15) >> 2, pp = l & 3, h = ((q >> 1) & 1) | ((g & 1) << 1);
#pragma unroll
        for (int T = 0; T < 2; T++)
          aw[T] = lb + (8 * g + q) * 128 + (((2 * (2 * ch + T) + (pp >> 1)) ^ (h << 1)) * 16) + 8 * (pp & 1);
      }
      f32x4 acc[4][2] = {};
      Frags F0, F1;
      BAR();
      STAMPC(2);
      load_frags(F0, ax, aw, 0, 0);
#pragma unroll
      for (int s = 0; s < 8; s++) {
        __builtin_amdgcn_sched_barrier(0);
        load_frags(F1, ax, aw, 1, (s % 3) * SLOT);
        mfma_block(acc, F0);
        sched_mix();
        __builtin_amdgcn_sched_barrier(0);
        BAR();
        if (s == 0 || s == 3 || s == 6) STAMPC(3 + s / 3);
        __builtin_amdgcn_sched_barrier(0);
        if (s < 7) load_frags(F0, ax, aw, 0, ((s + 1) % 3) * SLOT);
        mfma_block(acc, F1);
        if (s < 7) sched_mix();
      }
      __builtin_amdgcn_sched_barrier(0);
      STAMPC(6);
      const bool isF = ct < 8;
      const float c1 = isF ? 2.885390081777927f : -1.4426950408889634f;
      const float c2 = isF ? -2.f : 1.f, c3 = isF ? 1.f : 0.f;
      const u32 exb = (isF ? 0u : EXB) + (u32)((ct & 7) * 4 + ch * 2) * 1024u + (u32)(rt * 4) * 32768u;
#pragma unroll
      for (int j = 0; j < 4; j++) {
        u32 d[2][2];
#pragma unroll
        for (int T = 0; T < 2; T++) {
          float v[4];
#pragma unroll
          for (int e = 0; e < 4; e++) {
            const float t = __builtin_amdgcn_exp2f(acc[j][T][e] * c1);
            v[e] = __builtin_fmaf(__builtin_amdgcn_rcpf(t + 1.f), c2, c3);
          }
          d[T][0] = pk_bf16(v[0], v[1]); d[T][1] = pk_bf16(v[2], v[3]);
        }
        auto s0 = __builtin_amdgcn_permlane16_swap(d[0][0], d[1][0], false, false);
        auto s1 = __builtin_amdgcn_permlane16_swap(d[0][1], d[1][1], false, false);
        u32x4 v = {s0[0], s1[0], s0[1], s1[1]};
        const int rc = (j >> 1) * 2 + p;
        __builtin_amdgcn_raw_buffer_store_b128(v, rex, l * 16, exb + rc * 32768 + (j & 1) * 1024, AUXS);
      }
      STAMPC(7);
    }
    if (MODE != 0) {
      if (MODE == 2) wait_vm<0>();
      STAMPC(8);
      BAR();
      if (MODE == 2 && wave == 0) {
        const u64 tag = epoch + 1;
        if (l == 0) __hip_atomic_store(flags + ct, tag, __ATOMIC_RELAXED, __HIP_MEMORY_SCOPE_AGENT);
        int spins = 0;
        while (true) {
          u64 v = tag;
          if (l < 16) v = __hip_atomic_load(flags + l, __ATOMIC_RELAXED, __HIP_MEMORY_SCOPE_AGENT);
          if (__builtin_amdgcn_ballot_w64(v != tag) == 0) break;
          if (++spins > (1 << 20)) break;
          __builtin_amdgcn_s_sleep(1);
        }
        if (ct == 0 && l == 0) __hip_atomic_store(epw, tag, __ATOMIC_RELAXED, __HIP_MEMORY_SCOPE_AGENT);
      }
      STAMPC(9);
      BAR();
      const int rc = ct >> 2;
      const u32 base = (u32)(rt * 4 + rc) * 32768u;
      u32x4 av[4][2], bv[4][2];
#pragma unroll
      for (int q4 = 0; q4 < 4; q4++)
#pragma unroll
        for (int jl = 0; jl < 2; jl++) {
          const u32 o = base + (u32)(((q4 * 4 + wave) * 2 + jl) * 1024);
          av[q4][jl] = __builtin_amdgcn_raw_buffer_load_b128(rex, l * 16, o, AUXS);
          bv[q4][jl] = __builtin_amdgcn_raw_buffer_load_b128(rex, l * 16, o + EXB, AUXS);
        }
#pragma unroll
      for (int q4 = 0; q4 < 4; q4++) {
#pragma unroll
        for (int jl = 0; jl < 2; jl++) {
          u32x4 z;
#pragma unroll
          for (int e = 0; e < 4; e++) z[e] = mul_bf16x2(av[q4][jl][e], bv[q4][jl][e]);
          *LDSP(u32x4, lb + RE + ((q4 * 4 + wave) * 2 + jl) * 1024 + l * 16) = z;
        }
        BAR();
      }
      STAMPC(10);
    }
  }
  STAMP(31); STAMPC(30);
  if (wave == PADW) { if (tpadk == 0) tpadk = tpad0; const u64 tgt = tpadk + 4000; while (__builtin_amdgcn_s_memrealtime() < tgt) __builtin_amdgcn_s_sleep(1); }
}

extern "C" void kernel_launch(void* const* d_in, const int* in_sizes, int n_in, void* d_out, int out_size, void* d_ws,
                              size_t ws_size, hipStream_t stream) {
  const char* x = (const char*)d_in[0];
  const char* w = (const char*)d_in[1];
  const char* wo = (const char*)d_in[2];
  wn_kernel<2><<<256, 512, 0, stream>>>(x, w, wo, (char*)d_out, (char*)d_ws);
}
```

```cpp
#define PADK 20
#define PADW 4
#include <hip/hip_runtime.h>
#include <stdint.h>

typedef unsigned int u32;
typedef unsigned long long u64;
typedef __attribute__((address_space(3))) char* lds_t;
typedef float f32x4 __attribute__((ext_vector_type(4)));
typedef float f32x2 __attribute__((ext_vector_type(2)));
typedef __bf16 bf16x8 __attribute__((ext_vector_type(8)));
typedef __bf16 bf16x2 __attribute__((ext_vector_type(2)));
typedef short s16x4 __attribute__((ext_vector_type(4)));
typedef u32 u32x4 __attribute__((ext_vector_type(4)));
typedef u32 u32x2 __attribute__((ext_vector_type(2)));

#define FUSED 1

constexpr int XS = 17 * 1024, WS = 24 * 1024, SLOT = XS + WS;
constexpr u32 EXB = 16u * 128u * 512u * 2u;
constexpr u32 FLAG_OFF = 2u * EXB;
constexpr u32 EPOCH_OFF = FLAG_OFF + 4096;
constexpr u32 STAMP_OFF = FLAG_OFF + 65536;
constexpr int RA = 131072, RB = 98304, RC = 0, RD = 32768, RE = 65536;

#define LDSP(T, a) ((__attribute__((address_space(3))) T*)(uintptr_t)(a))
#define BAR() asm volatile("s_waitcnt lgkmcnt(0)\n\ts_barrier" ::: "memory")
template <int N> __device__ __forceinline__ void wait_vm() { asm volatile("s_waitcnt vmcnt(%0)" ::"n"(N) : "memory"); }

#define STAMP(i) do { if ((i) == 0) tpad0 = __builtin_amdgcn_s_memrealtime(); } while (0)
#define STAMPC(i) do { if ((i) == PADK) tpadk = __builtin_amdgcn_s_memrealtime(); } while (0)

struct Frags { bf16x8 x[6]; bf16x8 w[3][2]; };

__device__ __forceinline__ bf16x8 tr_frag(u32 a) {
  s16x4 lo = __builtin_amdgcn_ds_read_tr16_b64_v4i16(LDSP(s16x4, a));
  s16x4 hi = __builtin_amdgcn_ds_read_tr16_b64_v4i16(LDSP(s16x4, a + 512));
  typedef short s16x8 __attribute__((ext_vector_type(8)));
  s16x8 v = __builtin_shufflevector(lo, hi, 0, 1, 2, 3, 4, 5, 6, 7);
  return __builtin_bit_cast(bf16x8, v);
}
__device__ __forceinline__ bf16x8 tr_frag2(u32 a) {
  s16x4 lo = __builtin_amdgcn_ds_read_tr16_b64_v4i16(LDSP(s16x4, a));
  s16x4 hi = __builtin_amdgcn_ds_read_tr16_b64_v4i16(LDSP(s16x4, a + 1024));
  typedef short s16x8 __attribute__((ext_vector_type(8)));
  s16x8 v = __builtin_shufflevector(lo, hi, 0, 1, 2, 3, 4, 5, 6, 7);
  return __builtin_bit_cast(bf16x8, v);
}

__device__ __forceinline__ void load_frags(Frags& F, const u32 (&ax)[2][2], const u32 (&aw)[2], int ks, u32 sb) {
#pragma unroll
  for (int jj = 0; jj < 6; jj++) {
    const int win = jj >> 2, se = (2 * jj) & 7;
    F.x[jj] = *LDSP(bf16x8, ax[win][ks] + sb + se * 17 * 128);
  }
#pragma unroll
  for (int i = 0; i < 3; i++)
#pragma unroll
    for (int T = 0; T < 2; T++) F.w[i][T] = tr_frag(aw[T] + sb + XS + i * 8192 + ks * 4096);
}
__device__ __forceinline__ void mfma_block(f32x4 (&acc)[4][2], const Frags& F) {
#pragma unroll
  for (int i = 0; i < 3; i++)
#pragma unroll
    for (int j = 0; j < 4; j++)
#pragma unroll
      for (int T = 0; T < 2; T++)
        acc[j][T] = __builtin_amdgcn_mfma_f32_16x16x32_bf16(F.w[i][T], F.x[j - i + 2], acc[j][T], 0, 0, 0);
}
__device__ __forceinline__ void sched_mix() {
#pragma unroll
  for (int i = 0; i < 18; i++) {
    __builtin_amdgcn_sched_group_barrier(0x008, 1, 0);
    __builtin_amdgcn_sched_group_barrier(0x100, 1, 0);
  }
  __builtin_amdgcn_sched_group_barrier(0x008, 6, 0);
}
__device__ __forceinline__ u32 pk_bf16(float a, float b) {
  f32x2 p = {a, b};
  return __builtin_bit_cast(u32, __builtin_convertvector(p, bf16x2));
}
__device__ __forceinline__ u32 mul_bf16x2(u32 a, u32 b) {
  float al = __uint_as_float(a << 16), ah = __uint_as_float(a & 0xffff0000u);
  float bl = __uint_as_float(b << 16), bh = __uint_as_float(b & 0xffff0000u);
  return pk_bf16(al * bl, ah * bh);
}

template <int MODE>
__global__ __launch_bounds__(512, 2) void wn_kernel(const char* __restrict__ x, const char* __restrict__ w,
                                                    const char* __restrict__ wo, char* __restrict__ out,
                                                    char* __restrict__ ws) {
  __shared__ __attribute__((aligned(1024))) char lds_raw[163840];
  const u32 lb = (u32)(uintptr_t)(lds_t)lds_raw;
  const int tid = threadIdx.x, l = tid & 63;
  const int wave = __builtin_amdgcn_readfirstlane(tid >> 6);
  const int bid = blockIdx.x, xcd = bid & 7, idx = bid >> 3;
  const int rt = 8 * (xcd >> 2) + (idx >> 2), ct = 4 * (xcd & 3) + (idx & 3);
  const int g = l >> 4, r = l & 15;
  constexpr int AUXS = (MODE == 2) ? 16 : 0;
  u64 tpad0 = 0, tpadk = 0;
  STAMP(0); STAMPC(1);

  auto rex = __builtin_amdgcn_make_buffer_rsrc((void*)ws, (short)0, (int)(2u * EXB), 0x00020000);
  u64 epoch = 0;
  u64* flags = (u64*)(ws + FLAG_OFF + rt * 128);
  u64* epw = (u64*)(ws + EPOCH_OFF + rt * 128);

  if (wave >= 4) {
    const int L = wave - 4;
    auto rwo = __builtin_amdgcn_make_buffer_rsrc((void*)wo, (short)0, 512 * 1024, 0x00020000);
    const int cc = ct & 3;
    u32 voo[2];
#pragma unroll
    for (int e = 0; e < 2; e++) voo[e] = (u32)((l >> 4) * 1024 + cc * 256 + (((l & 15) ^ (2 * ((l >> 4) + 4 * e))) * 16));
    auto issue_wo = [&](int piece, int region) {
#pragma unroll
      for (int i = 0; i < 8; i++)
        __builtin_amdgcn_raw_ptr_buffer_load_lds(rwo, LDSP(void, lb + region + (L + 4 * i) * 1024), 16, voo[i & 1],
                                                 (piece * 128 + 4 * (L + 4 * i)) * 1024, 0, 0);
    };
    if (MODE != 1) {
      auto rx = __builtin_amdgcn_make_buffer_rsrc((void*)x, (short)0, 2000 * 1024, 0x00020000);
      auto rw = __builtin_amdgcn_make_buffer_rsrc((void*)w, (short)0, 1536 * 2048, 0x00020000);
      u32 vox[5];
#pragma unroll
      for (int i = 0; i < 5; i++) {
        const int pi = 8 * (L + 4 * i) + (l >> 3);
        const int s = pi / 17, rho = pi - 17 * s, u = 8 * rho + s;
        const int row = 125 * rt - 4 + u;
        const bool ok = (u >= 4 || (rt & 7) != 0) && row < 2000;
        vox[i] = ok ? (u32)(row * 1024 + (((l & 7) ^ (((rho >> 1) & 3) << 1)) * 16)) : 0x80000000u;
      }
      const int h = ((l >> 4) & 1) | ((L & 1) << 1);
      const u32 vow = (u32)((l >> 3) * 2048 + ct * 128 + (((l & 7) ^ (h << 1)) * 16));
      auto issue = [&](int s) {
        const u32 sb = lb + (s % 3) * SLOT;
#pragma unroll
        for (int i = 0; i < 4; i++)
          __builtin_amdgcn_raw_ptr_buffer_load_lds(rx, LDSP(void, sb + (L + 4 * i) * 1024), 16, vox[i], s * 128, 0, 0);
        if (L == 0) __builtin_amdgcn_raw_ptr_buffer_load_lds(rx, LDSP(void, sb + 16 * 1024), 16, vox[4], s * 128, 0, 0);
#pragma unroll
        for (int q = 0; q < 6; q++)
          __builtin_amdgcn_raw_ptr_buffer_load_lds(rw, LDSP(void, sb + XS + (L + 4 * q) * 1024), 16, vow,
                                                   ((q >> 1) * 512 + 8 * L + 32 * (q & 1) + s * 64) * 2048, 0, 0);
      };
#define WAITN(c, e) do { if (L == 0) wait_vm<11 * (c) + (e)>(); else wait_vm<10 * (c) + (e)>(); } while (0)
      { u64 tmp = (u64)x ^ (u64)w; asm volatile("" :: "s"(tmp)); asm volatile("s_waitcnt lgkmcnt(0)" ::: "memory"); }
      STAMPC(20);
      issue(0); STAMPC(21); issue(1); issue(2); STAMPC(22);
      WAITN(2, 0); STAMPC(2); BAR();
      WAITN(1, 0); STAMPC(3); BAR(); issue(3);
      WAITN(1, 0); STAMPC(4); BAR(); issue(4);
      WAITN(1, 0); STAMPC(5); BAR(); issue(5);
      WAITN(1, 0); STAMPC(6); BAR(); issue(6);
      WAITN(1, 0); STAMPC(7); BAR(); issue(7);
      if (MODE == 2) issue_wo(0, RA);
      if (MODE == 2) WAITN(1, 8); else WAITN(1, 0);
      STAMPC(8); BAR();
      if (MODE == 2) { issue_wo(1, RB); wait_vm<16>(); } else wait_vm<0>();
      STAMPC(9); BAR();
      if (MODE == 2) issue_wo(2, RC);
      BAR();
      if (MODE == 2) issue_wo(3, RD);
    } else {
      issue_wo(0, RA); issue_wo(1, RB); issue_wo(2, RC); issue_wo(3, RD);
    }
    if (MODE != 0) {
      wait_vm<0>(); STAMPC(10);
      BAR();
      bf16x8 wf[16][2];
      {
        const int q = (l & 15) >> 2, p = l & 3, pg = ((g & 1) << 1) | (g >> 1);
        u32 a[2];
#pragma unroll
        for (int T = 0; T < 2; T++)
          a[T] = lb + (8 * pg + q) * 256 + (((4 * L + 2 * T + (p >> 1)) ^ (2 * (q + 4 * (g & 1)))) * 16) + 8 * (p & 1);
#pragma unroll
        for (int k = 0; k < 16; k++) {
          const int reg = (k >> 2) == 0 ? RA : (k >> 2) == 1 ? RB : (k >> 2) == 2 ? RC : RD;
#pragma unroll
          for (int T = 0; T < 2; T++) wf[k][T] = tr_frag2(a[T] + reg + (k & 3) * 32 * 256);
        }
      }
      STAMPC(11);
      BAR();
      f32x4 acc[2][2] = {};
#pragma unroll
      for (int q4 = 0; q4 < 4; q4++) {
        BAR();
        bf16x8 zf[4][2];
#pragma unroll
        for (int k = 0; k < 4; k++)
#pragma unroll
          for (int jl = 0; jl < 2; jl++) zf[k][jl] = *LDSP(bf16x8, lb + RE + ((q4 * 4 + k) * 2 + jl) * 1024 + l * 16);
#pragma unroll
        for (int k = 0; k < 4; k++)
#pragma unroll
          for (int jl = 0; jl < 2; jl++)
#pragma unroll
            for (int T = 0; T < 2; T++)
              acc[jl][T] = __builtin_amdgcn_mfma_f32_16x16x32_bf16(wf[q4 * 4 + k][T], zf[k][jl], acc[jl][T], 0, 0, 0);
      }
      STAMPC(12);
      auto ro = __builtin_amdgcn_make_buffer_rsrc((void*)out, (short)0, 2000 * 1024, 0x00020000);
      const int rc = ct >> 2, p = rc & 1, jh = rc >> 1;
      const int cm = ((g & 1) << 4) | ((g >> 1) << 3);
#pragma unroll
      for (int jl = 0; jl < 2; jl++) {
        const int tl = 8 * r + 2 * (2 * jh + jl) + p;
        u32 x0 = pk_bf16(acc[jl][0][0], acc[jl][0][1]), x1 = pk_bf16(acc[jl][0][2], acc[jl][0][3]);
        u32 y0 = pk_bf16(acc[jl][1][0], acc[jl][1][1]), y1 = pk_bf16(acc[jl][1][2], acc[jl][1][3]);
        auto s0 = __builtin_amdgcn_permlane16_swap(x0, y0, false, false);
        auto s1 = __builtin_amdgcn_permlane16_swap(x1, y1, false, false);
        u32x4 v = {s0[0], s1[0], s0[1], s1[1]};
        const u32 off = tl < 125 ? (u32)((125 * rt + tl) * 1024 + (cc * 128 + 32 * L + cm) * 2) : 0x80000000u;
        __builtin_amdgcn_raw_buffer_store_b128(v, ro, off, 0, 0);
      }
      STAMPC(13);
    }
  } else {
    const int p = wave & 1, ch = wave >> 1;
    if (MODE == 2) epoch = __hip_atomic_load(epw, __ATOMIC_RELAXED, __HIP_MEMORY_SCOPE_AGENT);
    if (MODE != 1) {
      u32 ax[2][2], aw[2];
#pragma unroll
      for (int win = 0; win < 2; win++)
#pragma unroll
        for (int ks = 0; ks < 2; ks++) {
          const int rho = r + win;
          ax[win][ks] = lb + (p * 17 + rho) * 128 + (((4 * ks + g) ^ (((rho >> 1) & 3) << 1)) * 16);
        }
      {
        const int q = (l & 15) >> 2, pp = l & 3, h = ((q >> 1) & 1) | ((g & 1) << 1);
#pragma unroll
        for (int T = 0; T < 2; T++)
          aw[T] = lb + (8 * g + q) * 128 + (((2 * (2 * ch + T) + (pp >> 1)) ^ (h << 1)) * 16) + 8 * (pp & 1);
      }
      f32x4 acc[4][2] = {};
      Frags F0, F1;
      BAR();
      STAMPC(2);
      load_frags(F0, ax, aw, 0, 0);
#pragma unroll
      for (int s = 0; s < 8; s++) {
        __builtin_amdgcn_sched_barrier(0);
        load_frags(F1, ax, aw, 1, (s % 3) * SLOT);
        mfma_block(acc, F0);
        sched_mix();
        __builtin_amdgcn_sched_barrier(0);
        BAR();
        if (s == 0 || s == 3 || s == 6) STAMPC(3 + s / 3);
        __builtin_amdgcn_sched_barrier(0);
        if (s < 7) load_frags(F0, ax, aw, 0, ((s + 1) % 3) * SLOT);
        mfma_block(acc, F1);
        if (s < 7) sched_mix();
      }
      __builtin_amdgcn_sched_barrier(0);
      STAMPC(6);
      const bool isF = ct < 8;
      const float c1 = isF ? 2.885390081777927f : -1.4426950408889634f;
      const float c2 = isF ? -2.f : 1.f, c3 = isF ? 1.f : 0.f;
      const u32 exb = (isF ? 0u : EXB) + (u32)((ct & 7) * 4 + ch * 2) * 1024u + (u32)(rt * 4) * 32768u;
#pragma unroll
      for (int j = 0; j < 4; j++) {
        u32 d[2][2];
#pragma unroll
        for (int T = 0; T < 2; T++) {
          float v[4];
#pragma unroll
          for (int e = 0; e < 4; e++) {
            const float t = __builtin_amdgcn_exp2f(acc[j][T][e] * c1);
            v[e] = __builtin_fmaf(__builtin_amdgcn_rcpf(t + 1.f), c2, c3);
          }
          d[T][0] = pk_bf16(v[0], v[1]); d[T][1] = pk_bf16(v[2], v[3]);
        }
        auto s0 = __builtin_amdgcn_permlane16_swap(d[0][0], d[1][0], false, false);
        auto s1 = __builtin_amdgcn_permlane16_swap(d[0][1], d[1][1], false, false);
        u32x4 v = {s0[0], s1[0], s0[1], s1[1]};
        const int rc = (j >> 1) * 2 + p;
        __builtin_amdgcn_raw_buffer_store_b128(v, rex, l * 16, exb + rc * 32768 + (j & 1) * 1024, AUXS);
      }
      STAMPC(7);
    }
    if (MODE != 0) {
      if (MODE == 2) wait_vm<0>();
      STAMPC(8);
      BAR();
      if (MODE == 2 && wave == 0) {
        const u64 tag = epoch + 1;
        if (l == 0) __hip_atomic_store(flags + ct, tag, __ATOMIC_RELAXED, __HIP_MEMORY_SCOPE_AGENT);
        int spins = 0;
        while (true) {
          u64 v = tag;
          if (l < 16) v = __hip_atomic_load(flags + l, __ATOMIC_RELAXED, __HIP_MEMORY_SCOPE_AGENT);
          if (__builtin_amdgcn_ballot_w64(v != tag) == 0) break;
          if (++spins > (1 << 20)) break;
          __builtin_amdgcn_s_sleep(1);
        }
        if (ct == 0 && l == 0) __hip_atomic_store(epw, tag, __ATOMIC_RELAXED, __HIP_MEMORY_SCOPE_AGENT);
      }
      STAMPC(9);
      BAR();
      const int rc = ct >> 2;
      const u32 base = (u32)(rt * 4 + rc) * 32768u;
      u32x4 av[4][2], bv[4][2];
#pragma unroll
      for (int q4 = 0; q4 < 4; q4++)
#pragma unroll
        for (int jl = 0; jl < 2; jl++) {
          const u32 o = base + (u32)(((q4 * 4 + wave) * 2 + jl) * 1024);
          av[q4][jl] = __builtin_amdgcn_raw_buffer_load_b128(rex, l * 16, o, AUXS);
          bv[q4][jl] = __builtin_amdgcn_raw_buffer_load_b128(rex, l * 16, o + EXB, AUXS);
        }
#pragma unroll
      for (int q4 = 0; q4 < 4; q4++) {
#pragma unroll
        for (int jl = 0; jl < 2; jl++) {
          u32x4 z;
#pragma unroll
          for (int e = 0; e < 4; e++) z[e] = mul_bf16x2(av[q4][jl][e], bv[q4][jl][e]);
          *LDSP(u32x4, lb + RE + ((q4 * 4 + wave) * 2 + jl) * 1024 + l * 16) = z;
        }
        BAR();
      }
      STAMPC(10);
    }
  }
  STAMP(31); STAMPC(30);
  if (wave == PADW) { if (tpadk == 0) tpadk = tpad0; const u64 tgt = tpadk + 4000; while (__builtin_amdgcn_s_memrealtime() < tgt) __builtin_amdgcn_s_sleep(1); }
}

extern "C" void kernel_launch(void* const* d_in, const int* in_sizes, int n_in, void* d_out, int out_size, void* d_ws,
                              size_t ws_size, hipStream_t stream) {
  const char* x = (const char*)d_in[0];
  const char* w = (const char*)d_in[1];
  const char* wo = (const char*)d_in[2];
  wn_kernel<2><<<256, 512, 0, stream>>>(x, w, wo, (char*)d_out, (char*)d_ws);
}
```

```cpp
#define PFMODE 5
#define PS -5
#define PCS 1
#define PCD 3
#define NPRE 3
#define OUTAUX 16
#define XC 0
#include <hip/hip_runtime.h>
#include <stdint.h>

typedef unsigned int u32;
typedef unsigned long long u64;
typedef __attribute__((address_space(3))) char* lds_t;
typedef float f32x4 __attribute__((ext_vector_type(4)));
typedef float f32x2 __attribute__((ext_vector_type(2)));
typedef __bf16 bf16x8 __attribute__((ext_vector_type(8)));
typedef __bf16 bf16x2 __attribute__((ext_vector_type(2)));
typedef short s16x4 __attribute__((ext_vector_type(4)));
typedef u32 u32x4 __attribute__((ext_vector_type(4)));
typedef u32 u32x2 __attribute__((ext_vector_type(2)));

#define FUSED 1

constexpr int XS = 17 * 1024, WS = 24 * 1024, SLOT = XS + WS;
constexpr u32 EXB = 16u * 128u * 512u * 2u;
constexpr u32 FLAG_OFF = 2u * EXB;
constexpr u32 EPOCH_OFF = FLAG_OFF + 4096;
#define JMAJ 1
#define JV 7
#define LDPRE 0
#define PFD 0
#define LDAUX 16
#define QORD 0, 2, 3, 1
constexpr int RA = 131072, RB = 98304, RC = 0, RD = 32768, RE = 65536;

#define ROT 1
#define PFCODE 1
constexpr int PFS = 3 * SLOT;
constexpr int CODE_LEN = 15872;
#define LDSP(T, a) ((__attribute__((address_space(3))) T*)(uintptr_t)(a))
#define BAR() asm volatile("s_waitcnt lgkmcnt(0)\n\ts_barrier" ::: "memory")
template <int N> __device__ __forceinline__ void wait_vm() { asm volatile("s_waitcnt vmcnt(%0)" ::"n"(N) : "memory"); }

struct Frags { bf16x8 x[6]; bf16x8 w[3][2]; };

__device__ __forceinline__ bf16x8 tr_frag(u32 a) {
  s16x4 lo = __builtin_amdgcn_ds_read_tr16_b64_v4i16(LDSP(s16x4, a));
  s16x4 hi = __builtin_amdgcn_ds_read_tr16_b64_v4i16(LDSP(s16x4, a + 512));
  typedef short s16x8 __attribute__((ext_vector_type(8)));
  s16x8 v = __builtin_shufflevector(lo, hi, 0, 1, 2, 3, 4, 5, 6, 7);
  return __builtin_bit_cast(bf16x8, v);
}
__device__ __forceinline__ bf16x8 tr_frag2(u32 a) {
  s16x4 lo = __builtin_amdgcn_ds_read_tr16_b64_v4i16(LDSP(s16x4, a));
  s16x4 hi = __builtin_amdgcn_ds_read_tr16_b64_v4i16(LDSP(s16x4, a + 1024));
  typedef short s16x8 __attribute__((ext_vector_type(8)));
  s16x8 v = __builtin_shufflevector(lo, hi, 0, 1, 2, 3, 4, 5, 6, 7);
  return __builtin_bit_cast(bf16x8, v);
}

__device__ __forceinline__ void load_frags(Frags& F, const u32 (&ax)[2][2], const u32 (&aw)[2], int ks, u32 sb) {
#pragma unroll
  for (int jj = 0; jj < 6; jj++) {
    const int win = jj >> 2, se = (2 * jj) & 7;
    F.x[jj] = *LDSP(bf16x8, ax[win][ks] + sb + se * 17 * 128);
  }
#pragma unroll
  for (int i = 0; i < 3; i++)
#pragma unroll
    for (int T = 0; T < 2; T++) F.w[i][T] = tr_frag(aw[T] + sb + XS + i * 8192 + ks * 4096);
}
__device__ __forceinline__ void mfma_block(f32x4 (&acc)[4][2], const Frags& F) {
#pragma unroll
  for (int i = 0; i < 3; i++)
#pragma unroll
    for (int j = 0; j < 4; j++)
#pragma unroll
      for (int T = 0; T < 2; T++)
        acc[j][T] = __builtin_amdgcn_mfma_f32_16x16x32_bf16(F.w[i][T], F.x[j - i + 2], acc[j][T], 0, 0, 0);
}
__device__ __forceinline__ void mfma_row(f32x4 (&acc)[4][2], const Frags& F, int j) {
#pragma unroll
  for (int i = 0; i < 3; i++)
#pragma unroll
    for (int T = 0; T < 2; T++)
      acc[j][T] = __builtin_amdgcn_mfma_f32_16x16x32_bf16(F.w[i][T], F.x[j - i + 2], acc[j][T], 0, 0, 0);
}
__device__ __forceinline__ void sched_mix() {
#pragma unroll
  for (int i = 0; i < 18; i++) {
    __builtin_amdgcn_sched_group_barrier(0x008, 1, 0);
    __builtin_amdgcn_sched_group_barrier(0x100, 1, 0);
  }
  __builtin_amdgcn_sched_group_barrier(0x008, 6, 0);
}
__device__ __forceinline__ u32 pk_bf16(float a, float b) {
  f32x2 p = {a, b};
  return __builtin_bit_cast(u32, __builtin_convertvector(p, bf16x2));
}
typedef _Float16 f16x2 __attribute__((ext_vector_type(2)));
__device__ __forceinline__ u32 pk_f16(float a, float b) {
  f32x2 p = {a, b};
  return __builtin_bit_cast(u32, __builtin_convertvector(p, f16x2));
}
__device__ __forceinline__ u32x4 mul_f16x8(u32x4 a, u32x4 b) {
  float p[8];
  asm("v_fma_mix_f32 %0, %8, %12, 0 op_sel_hi:[1,1,0]\n\t"
      "v_fma_mix_f32 %1, %8, %12, 0 op_sel:[1,1,0] op_sel_hi:[1,1,0]\n\t"
      "v_fma_mix_f32 %2, %9, %13, 0 op_sel_hi:[1,1,0]\n\t"
      "v_fma_mix_f32 %3, %9, %13, 0 op_sel:[1,1,0] op_sel_hi:[1,1,0]\n\t"
      "v_fma_mix_f32 %4, %10, %14, 0 op_sel_hi:[1,1,0]\n\t"
      "v_fma_mix_f32 %5, %10, %14, 0 op_sel:[1,1,0] op_sel_hi:[1,1,0]\n\t"
      "v_fma_mix_f32 %6, %11, %15, 0 op_sel_hi:[1,1,0]\n\t"
      "v_fma_mix_f32 %7, %11, %15, 0 op_sel:[1,1,0] op_sel_hi:[1,1,0]"
      : "=&v"(p[0]), "=&v"(p[1]), "=&v"(p[2]), "=&v"(p[3]), "=&v"(p[4]), "=&v"(p[5]), "=&v"(p[6]), "=&v"(p[7])
      : "v"(a[0]), "v"(a[1]), "v"(a[2]), "v"(a[3]), "v"(b[0]), "v"(b[1]), "v"(b[2]), "v"(b[3]));
  u32x4 z = {pk_bf16(p[0], p[1]), pk_bf16(p[2], p[3]), pk_bf16(p[4], p[5]), pk_bf16(p[6], p[7])};
  return z;
}
__device__ __forceinline__ u32 max4(u32 m, u32x4 v) {
  m = __builtin_elementwise_max(__builtin_elementwise_max(m, v[0]), v[1]);
  return __builtin_elementwise_max(__builtin_elementwise_max(m, v[2]), v[3]);
}

#define K0EXPR (2 * (xcd & 3) + 4 * (xcd >> 2))
__device__ __forceinline__ void calc_vox(u32 (&vox)[5], int L, int l, int rt) {
#pragma unroll
  for (int i = 0; i < 5; i++) {
    const int pi = 8 * (L + 4 * i) + (l >> 3);
    const int s = (pi * 241) >> 12, rho = pi - 17 * s, u = 8 * rho + s;
    const int row = 125 * rt - 4 + u;
    const bool ok = (u >= 4 || (rt & 7) != 0) && row < 2000;
    vox[i] = ok ? (u32)(row * 1024 + (((l & 7) ^ (((rho >> 1) & 3) << 1)) * 16)) : 0x80000000u;
  }
}

__global__ __launch_bounds__(512, 2) void wn_kernel(const char* __restrict__ x, const char* __restrict__ w,
                                                    const char* __restrict__ wo, char* __restrict__ out,
                                                    char* __restrict__ ws) {
  __shared__ __attribute__((aligned(1024))) char lds_raw[163840];
  const u32 lb = (u32)(uintptr_t)(lds_t)lds_raw;
  const int tid = threadIdx.x, l = tid & 63;
  const int wave = __builtin_amdgcn_readfirstlane(tid >> 6);
  const int bid = blockIdx.x, xcd = bid & 7, idx = bid >> 3;
  const int rt = 8 * (xcd >> 2) + (idx >> 2), ct = 4 * (xcd & 3) + (idx & 3);
  const int g = l >> 4, r = l & 15;
  u64 pc0;
  asm volatile("s_getpc_b64 %0" : "=s"(pc0));

  if (wave < 4) {
    const int p = wave & 1, ch = wave >> 1;
    auto rex = __builtin_amdgcn_make_buffer_rsrc((void*)ws, (short)0, (int)(2u * EXB), 0x00020000);
    u64* flags = (u64*)(ws + FLAG_OFF + rt * 128);
    u64* epw = (u64*)(ws + EPOCH_OFF + rt * 128);
    const u64 tag = __hip_atomic_load(epw, __ATOMIC_RELAXED, __HIP_MEMORY_SCOPE_AGENT) + 1;
    const bool isF = ct < 8;
    const u32 exb = (isF ? 0u : EXB) + (u32)((ct & 7) * 4 + ch * 2) * 1024u + (u32)(rt * 4) * 32768u;
    u32 ax[2][2], aw[2];
#pragma unroll
    for (int win = 0; win < 2; win++)
#pragma unroll
      for (int ks = 0; ks < 2; ks++) {
        const int rho = r + win;
        ax[win][ks] = lb + (p * 17 + rho) * 128 + (((4 * ks + g) ^ (((rho >> 1) & 3) << 1)) * 16);
      }
    {
      const int q = (l & 15) >> 2, pp = l & 3, h = ((q >> 1) & 1) | ((g & 1) << 1);
#pragma unroll
      for (int T = 0; T < 2; T++)
        aw[T] = lb + (8 * g + q) * 128 + (((2 * (2 * ch + T) + (pp >> 1)) ^ (h << 1)) * 16) + 8 * (pp & 1);
    }
    f32x4 acc[4][2] = {};
    Frags F0, F1;
    BAR();
    load_frags(F0, ax, aw, 0, 0);
#pragma unroll
    for (int s = 0; s < 8; s++) {
      __builtin_amdgcn_sched_barrier(0);
      load_frags(F1, ax, aw, 1, (s % 3) * SLOT);
      mfma_block(acc, F0);
      sched_mix();
      __builtin_amdgcn_sched_barrier(0);
      if (XC && s == 0) wait_vm<0>();
      if (PCS >= 0 && s == PCS + PCD) wait_vm<0>();
      BAR();
      if (s == (PCS >= 0 ? PCS + PCD : PS + 1) && wave == 0 && l == 0)
        __hip_atomic_store(flags + ct, tag, __ATOMIC_RELAXED, __HIP_MEMORY_SCOPE_AGENT);
      if (PCS >= 0 && s == PCS) {
        const u32x4 pz = {~0u, ~0u, ~0u, ~0u};
#pragma unroll
        for (int j = 0; j < 4; j++)
          __builtin_amdgcn_raw_buffer_store_b128(pz, rex, l * 16, exb + ((j >> 1) * 2 + p) * 32768 + (j & 1) * 1024, 16);
        asm volatile("s_nop 1" ::"v"(pz) : "memory");
      }

      __builtin_amdgcn_sched_barrier(0);
      if (s < 7) load_frags(F0, ax, aw, 0, ((s + 1) % 3) * SLOT);
      if (s < 7 || !JMAJ) mfma_block(acc, F1);
      if (s < 7) sched_mix();
    }
    __builtin_amdgcn_sched_barrier(0);
    u64 fl;
    u64* flp = flags + (l & 15);
    asm volatile("global_load_dwordx2 %0, %1, off sc1" : "=v"(fl) : "v"(flp) : "memory");
    __builtin_amdgcn_sched_barrier(0);
    const float c1 = isF ? 2.885390081777927f : -1.4426950408889634f;
    const float c2 = isF ? -2.f : 1.f, c3 = isF ? 1.f : 0.f;
#pragma unroll
    for (int j = 0; j < 4; j++) {
      if (JMAJ) {
        if (j == 0) { mfma_row(acc, F1, 0); __builtin_amdgcn_sched_barrier(0); }
        if (j < 3) mfma_row(acc, F1, j + 1);
      }
      u32 d[2][2];
#pragma unroll
      for (int T = 0; T < 2; T++) {
        float v[4];
#pragma unroll
        for (int e = 0; e < 4; e++) {
          const float t = __builtin_amdgcn_exp2f(acc[j][T][e] * c1);
          v[e] = __builtin_fmaf(__builtin_amdgcn_rcpf(t + 1.f), c2, c3);
        }
        d[T][0] = __builtin_elementwise_min(pk_f16(v[0], v[1]), 0xfffffffeu);
        d[T][1] = __builtin_elementwise_min(pk_f16(v[2], v[3]), 0xfffffffeu);
      }
      auto s0 = __builtin_amdgcn_permlane16_swap(d[0][0], d[1][0], false, false);
      auto s1 = __builtin_amdgcn_permlane16_swap(d[0][1], d[1][1], false, false);
      u32x4 v = {s0[0], s1[0], s0[1], s1[1]};
      if (JMAJ && j < 3) {
#pragma unroll
        for (int i = 0; i < 6; i++) {
          __builtin_amdgcn_sched_group_barrier(0x008, 1, 0);
          __builtin_amdgcn_sched_group_barrier(0x002, JV, 0);
        }
      }
      __builtin_amdgcn_raw_buffer_store_b128(v, rex, l * 16, exb + ((j >> 1) * 2 + p) * 32768 + (j & 1) * 1024, 16);
      asm volatile("s_nop 1" ::"v"(v) : "memory");
      if (JMAJ) __builtin_amdgcn_sched_barrier(0);
      if (j == 1) {
        __builtin_amdgcn_sched_barrier(0);
        asm volatile("s_barrier" ::: "memory");
        __builtin_amdgcn_sched_barrier(0);
      }
    }
    __builtin_amdgcn_sched_barrier(0);
    {
      asm volatile("s_waitcnt vmcnt(4)" : "+v"(fl)::"memory");
      if (__builtin_expect(__builtin_amdgcn_ballot_w64(fl != tag) != 0, 0)) {
        int spins = 0;
        do {
          fl = __hip_atomic_load(flp, __ATOMIC_RELAXED, __HIP_MEMORY_SCOPE_AGENT);
        } while (__builtin_amdgcn_ballot_w64(fl != tag) != 0 && ++spins < (1 << 20));
      }
      if (wave == 0 && ct == 0 && l == 0) __hip_atomic_store(epw, tag, __ATOMIC_RELAXED, __HIP_MEMORY_SCOPE_AGENT);
    }
    asm volatile("" ::: "memory");
    __builtin_amdgcn_sched_barrier(0);
    const int rc = ct >> 2;
    const u32 base = (u32)(rt * 4 + rc) * 32768u + (u32)wave * 2048u;
    constexpr int qo[4] = {QORD};
    u32x4 av[4][2], bv[4][2];
    auto ld = [&](int q4) {
#pragma unroll
      for (int jl = 0; jl < 2; jl++) {
        const u32 o = base + (u32)(qo[q4] * 8192 + jl * 1024);
        av[q4][jl] = __builtin_amdgcn_raw_buffer_load_b128(rex, l * 16, o, LDAUX);
        bv[q4][jl] = __builtin_amdgcn_raw_buffer_load_b128(rex, l * 16, o + EXB, LDAUX);
      }
    };
#pragma unroll
    for (int q4 = 0; q4 < NPRE; q4++) ld(q4);
    __builtin_amdgcn_sched_barrier(0);
#pragma unroll
    for (int q4 = 0; q4 < 4; q4++) {
      auto bad = [&]() {
        const u32 m = max4(max4(max4(max4(0u, av[q4][0]), bv[q4][0]), av[q4][1]), bv[q4][1]);
        return __builtin_amdgcn_ballot_w64(m == ~0u) != 0;
      };
      if (__builtin_expect(bad(), 0)) {
        int spins = 0;
        do {
          asm volatile("" ::: "memory");
          ld(q4);
        } while (bad() && ++spins < (1 << 16));
      }
#pragma unroll
      for (int jl = 0; jl < 2; jl++) {
        const u32x4 z = mul_f16x8(av[q4][jl], bv[q4][jl]);
        *LDSP(u32x4, lb + RE + ((q4 * 4 + wave) * 2 + jl) * 1024 + l * 16) = z;
      }
      if (LDPRE && q4 + NPRE < 4) ld(q4 + NPRE);
      __builtin_amdgcn_sched_barrier(0);
      BAR();
      if (!LDPRE && q4 + NPRE < 4) ld(q4 + NPRE);
      __builtin_amdgcn_sched_barrier(0);
    }
  } else {
    const int L = wave - 4;
    auto rx = __builtin_amdgcn_make_buffer_rsrc((void*)x, (short)0, 2000 * 1024, 0x00020000);
    auto rw = __builtin_amdgcn_make_buffer_rsrc((void*)w, (short)0, 1536 * 2048, 0x00020000);
    const int h = ((l >> 4) & 1) | ((L & 1) << 1);
    const u32 vow = (u32)((l >> 3) * 2048 + ct * 128 + (((l & 7) ^ (h << 1)) * 16));
    u32 vox[5];
    if (!XC) calc_vox(vox, L, l, rt);
    const int k0 = K0EXPR;
    auto issue = [&](int s) {
      const u32 sb = lb + (s % 3) * SLOT;
      const int kb = (s + k0) & 7;
#pragma unroll
      for (int q = 0; q < 6; q++)
        __builtin_amdgcn_raw_ptr_buffer_load_lds(rw, LDSP(void, sb + XS + (L + 4 * q) * 1024), 16, vow,
                                                 ((q >> 1) * 512 + 8 * L + 32 * (q & 1) + kb * 64) * 2048, 0, 0);
      if (s == 0 || (XC && s == 1)) __builtin_amdgcn_sched_barrier(0);
      if (XC && s < 2) return;
#pragma unroll
      for (int i = 0; i < 4; i++)
        __builtin_amdgcn_raw_ptr_buffer_load_lds(rx, LDSP(void, sb + (L + 4 * i) * 1024), 16, vox[i], kb * 128, 0, 0);
      if (L == 0) __builtin_amdgcn_raw_ptr_buffer_load_lds(rx, LDSP(void, sb + 16 * 1024), 16, vox[4], kb * 128, 0, 0);
      if (s < 2) __builtin_amdgcn_sched_barrier(0);
    };
    auto rwo = __builtin_amdgcn_make_buffer_rsrc((void*)wo, (short)0, 512 * 1024, 0x00020000);
    const int cc = ct & 3;
    u32 voo[2];
#pragma unroll
    for (int e = 0; e < 2; e++) voo[e] = (u32)((l >> 4) * 1024 + cc * 256 + (((l & 15) ^ (2 * ((l >> 4) + 4 * e))) * 16));
    auto issue_wo = [&](int piece, int region) {
#pragma unroll
      for (int i = 0; i < 8; i++)
        __builtin_amdgcn_raw_ptr_buffer_load_lds(rwo, LDSP(void, lb + region + (L + 4 * i) * 1024), 16, voo[i & 1],
                                                 (piece * 128 + 4 * (L + 4 * i)) * 1024, 0, 0);
    };
#define WAITN(c, e) do { if (L == 0) wait_vm<11 * (c) + (e)>(); else wait_vm<10 * (c) + (e)>(); } while (0)
    const u32 pfl = lb + PFS + wave * 256;
    auto poison = [&]() {
      auto rex = __builtin_amdgcn_make_buffer_rsrc((void*)ws, (short)0, (int)(2u * EXB), 0x00020000);
      const u32 exb = (ct < 8 ? 0u : EXB) + (u32)((ct & 7) * 4 + (L >> 1) * 2) * 1024u + (u32)(rt * 4 + (L & 1)) * 32768u;
      const u32x4 pz = {~0u, ~0u, ~0u, ~0u};
#pragma unroll
      for (int j = 0; j < 4; j++)
        __builtin_amdgcn_raw_buffer_store_b128(pz, rex, l * 16, exb + (j >> 1) * 65536 + (j & 1) * 1024, 16);
      asm volatile("s_nop 1" ::"v"(pz) : "memory");
    };
#define STEP(k, extra) do { issue((k) + 2); if (PS == (k)) poison(); extra; } while (0)
#define PE(k) (PS == (k) ? 4 : 0)
    {
      const bool isx = idx < 16;
      const int G = idx * 64 + L * 16 + (l & 15), H = G - 1024;
      const u32 vx = G < 1000 ? (u32)((1000 * (xcd >> 2) + G) * 1024) : 0x80000000u;
      const u32 vw = (H >> 2) < 192 ? (u32)(((H >> 8) * 512 + ((H >> 2) & 63)) * 2048 + (4 * (xcd & 3) + (G & 3)) * 128) : 0x80000000u;
      const u32 vpf = l < 16 ? (isx ? vx : vw) : 0x80000000u;
      auto rp = isx ? rx : rw;
      const int pstr = isx ? 128 : 131072;
      auto pq = [&](int s0) { __builtin_amdgcn_raw_ptr_buffer_load_lds(rp, LDSP(void, pfl), 4, vpf, ((s0 + k0) & 7) * pstr, 0, 0); };
      auto rc = __builtin_amdgcn_make_buffer_rsrc((void*)(pc0 & ~127ull), (short)0, CODE_LEN, 0x00020000);
      if (PFCODE) __builtin_amdgcn_raw_ptr_buffer_load_lds(rc, LDSP(void, pfl), 4, l == 0 ? (u32)(idx * 4 + L) * 128u : 0x80000000u, 0, 0, 0);
      issue(0); issue(1); if (PFD) __builtin_amdgcn_s_sleep(PFD); pq(2); pq(3);
      if (XC) {
        __builtin_amdgcn_sched_barrier(0);
        int lx = l;
        asm volatile("" : "+v"(lx));
        calc_vox(vox, L, lx, rt);
        __builtin_amdgcn_sched_barrier(0);
        wait_vm<8>();
      } else WAITN(1, 2); BAR(); STEP(0, pq(4));
      WAITN(1, 3 + PE(0)); BAR(); STEP(1, pq(5));
      WAITN(1, 2 + PE(1)); BAR(); STEP(2, pq(6));
      WAITN(1, 2 + PE(2)); BAR(); STEP(3, pq(7));
      __builtin_amdgcn_raw_ptr_buffer_load_lds(rwo, LDSP(void, pfl), 4, l < 32 ? (u32)(idx * 128 + L * 32 + l) * 128u : 0x80000000u, 0, 0, 0);
      WAITN(1, 3 + PE(3)); BAR(); STEP(4, );
      WAITN(1, 2 + PE(4)); BAR(); issue(7);
      issue_wo(0, RA);
      WAITN(1, 8);
    } BAR();
    issue_wo(1, RB); wait_vm<16>(); BAR();
    issue_wo(2, RC);
    BAR();
    issue_wo(3, RD);
    wait_vm<0>();
    BAR();
    constexpr int qo[4] = {QORD};
    bf16x8 wf[16][2];
    {
      const int q = (l & 15) >> 2, p = l & 3, pg = ((g & 1) << 1) | (g >> 1);
      u32 a[2];
#pragma unroll
      for (int T = 0; T < 2; T++)
        a[T] = lb + (8 * pg + q) * 256 + (((4 * L + 2 * T + (p >> 1)) ^ (2 * (q + 4 * (g & 1)))) * 16) + 8 * (p & 1);
#pragma unroll
      for (int q4 = 0; q4 < 4; q4++)
#pragma unroll
        for (int kk = 0; kk < 4; kk++) {
          const int k = qo[q4] * 4 + kk;
          const int reg = (k >> 2) == 0 ? RA : (k >> 2) == 1 ? RB : (k >> 2) == 2 ? RC : RD;
#pragma unroll
          for (int T = 0; T < 2; T++) wf[k][T] = tr_frag2(a[T] + reg + (k & 3) * 32 * 256);
        }
    }
    f32x4 acc[2][2] = {};
#pragma unroll
    for (int q4 = 0; q4 < 4; q4++) {
      BAR();
      bf16x8 zf[4][2];
#pragma unroll
      for (int k = 0; k < 4; k++)
#pragma unroll
        for (int jl = 0; jl < 2; jl++) zf[k][jl] = *LDSP(bf16x8, lb + RE + ((q4 * 4 + k) * 2 + jl) * 1024 + l * 16);
#pragma unroll
      for (int k = 0; k < 4; k++)
#pragma unroll
        for (int jl = 0; jl < 2; jl++)
#pragma unroll
          for (int T = 0; T < 2; T++)
            acc[jl][T] = __builtin_amdgcn_mfma_f32_16x16x32_bf16(wf[qo[q4] * 4 + k][T], zf[k][jl], acc[jl][T], 0, 0, 0);
    }
    auto ro = __builtin_amdgcn_make_buffer_rsrc((void*)out, (short)0, 2000 * 1024, 0x00020000);
    const int rc = ct >> 2, p = rc & 1, jh = rc >> 1;
    const int cm = ((g & 1) << 4) | ((g >> 1) << 3);
#pragma unroll
    for (int jl = 0; jl < 2; jl++) {
      const int tl = 8 * r + 2 * (2 * jh + jl) + p;
      u32 x0 = pk_bf16(acc[jl][0][0], acc[jl][0][1]), x1 = pk_bf16(acc[jl][0][2], acc[jl][0][3]);
      u32 y0 = pk_bf16(acc[jl][1][0], acc[jl][1][1]), y1 = pk_bf16(acc[jl][1][2], acc[jl][1][3]);
      auto s0 = __builtin_amdgcn_permlane16_swap(x0, y0, false, false);
      auto s1 = __builtin_amdgcn_permlane16_swap(x1, y1, false, false);
      u32x4 v = {s0[0], s1[0], s0[1], s1[1]};
      const u32 off = tl < 125 ? (u32)((125 * rt + tl) * 1024 + (cc * 128 + 32 * L + cm) * 2) : 0x80000000u;
      __builtin_amdgcn_raw_buffer_store_b128(v, ro, off, 0, OUTAUX);
    }
  }
}

extern "C" void kernel_launch(void* const* d_in, const int* in_sizes, int n_in, void* d_out, int out_size, void* d_ws,
                              size_t ws_size, hipStream_t stream) {
  wn_kernel<<<256, 512, 0, stream>>>((const char*)d_in[0], (const char*)d_in[1], (const char*)d_in[2], (char*)d_out,
                                     (char*)d_ws);
}
```
